```python
import torch

def _softmax(x, axis=-1):
    return torch.softmax(x, dim=axis)
def _gelu(x):
    return torch.nn.functional.gelu(x, approximate="tanh")
def _silu(x):
    return torch.nn.functional.silu(x)
def _sigmoid(x):
    return torch.sigmoid(x)
def _layernorm(x, axis=-1, eps=1e-5):
    m = x.mean(dim=axis, keepdim=True)
    v = x.var(dim=axis, keepdim=True, unbiased=False)
    return (x - m) / torch.sqrt(v + eps)
def _scatter_add(tgt, idx, src, axis=0):
    return tgt.clone().index_add_(axis, idx, src)
def _segment_sum(src, seg, n):
    out = torch.zeros((n,) + src.shape[1:], dtype=src.dtype)
    return out.index_add_(0, seg, src)
def _pad(x, pw):
    flat = tuple(v for pair in reversed(pw) for v in pair)
    return torch.nn.functional.pad(x, flat)

kernel_name = 'standalone__gqa_softmax__sampling'

def setup_inputs():

    _w = (lambda a: (lambda t: t.to(torch.bfloat16) if t.is_floating_point() else t)(torch.as_tensor(a)))
    B, Hq, Hkv, Sq, Sk, D = 8, 32, 16, 16, 8192, 128
    return {
        "q": _w((lambda *s: torch.randn(*s, dtype=torch.bfloat16) * 0.8)(B, Hq, Sq, D)),
        "k": _w((lambda *s: torch.randn(*s, dtype=torch.bfloat16) * 0.8)(B, Hkv, Sk, D)),
        "v": _w((lambda *s: torch.randn(*s, dtype=torch.bfloat16) * 0.05)(B, Hkv, Sk, D)),
    }

def reference(q, k, v):
    B, Hq, Sq, D = q.shape
    Hkv = k.shape[1]
    rep = Hq // Hkv
    k = (lambda x, n, axis: torch.repeat_interleave(x, n, dim=axis))(k, rep, axis=1)
    v = (lambda x, n, axis: torch.repeat_interleave(x, n, dim=axis))(v, rep, axis=1)
    scale = D ** -0.5
    scores = torch.einsum("bhqd,bhkd->bhqk", q, k) * scale
    p = _softmax(scores, axis=-1)
    return torch.einsum("bhqk,bhkd->bhqd", p, v)
```

```cpp
#include <hip/hip_runtime.h>
#include <hip/hip_bf16.h>
#include <stdint.h>

#define KW 4
#define KNS 4
#define KPAT 0

namespace {

constexpr int kB = 8, kHQ = 32, kHKV = 16, kSQ = 16, kSK = 8192, kD = 128;
constexpr int NHEAD = kB * kHKV;
constexpr int W = KW, NS = KNS;
constexpr int ROUNDS = kSK / (32 * W);
constexpr int NQD = 16 / W;
constexpr unsigned HEAD_BYTES = kSK * kD * 2;
constexpr float C2 = 0.08838834764831845f * 1.4426950408889634f;

constexpr int RING = NS * 8192;
constexpr int LDS_Q = W * RING;
constexpr int LDS_ST = LDS_Q + 8192;
constexpr int LDS_BYTES = LDS_ST + W * 256;
static_assert(RING >= 16384 && LDS_BYTES <= 163840, "LDS map");
static_assert(8 * NS - 4 <= 63, "vmcnt is 6 bits");

constexpr size_t WS_PO = 0;
constexpr size_t WS_PS = WS_PO + (size_t)NHEAD * 2 * 16384;

typedef short bf16x8 __attribute__((ext_vector_type(8)));
typedef short s16x4 __attribute__((ext_vector_type(4)));
typedef float f32x16 __attribute__((ext_vector_type(16)));
typedef float f32x4 __attribute__((ext_vector_type(4)));
typedef float f32x2 __attribute__((ext_vector_type(2)));
typedef unsigned u32x4 __attribute__((ext_vector_type(4)));
typedef __bf16 bf16x2_t __attribute__((ext_vector_type(2)));
#define LAS __attribute__((address_space(3)))
typedef LAS char* lds_ptr;

__device__ __forceinline__ unsigned pack_bf16(float lo, float hi) {
  f32x2 v = {lo, hi};
  return __builtin_bit_cast(unsigned, __builtin_convertvector(v, bf16x2_t));
}
__device__ __forceinline__ float swap_max(float x) {
  auto r = __builtin_amdgcn_permlane32_swap(__float_as_uint(x), __float_as_uint(x), false, false);
  return fmaxf(__uint_as_float(r[0]), __uint_as_float(r[1]));
}
__device__ __forceinline__ float swap_sum(float x) {
  auto r = __builtin_amdgcn_permlane32_swap(__float_as_uint(x), __float_as_uint(x), false, false);
  return __uint_as_float(r[0]) + __uint_as_float(r[1]);
}
__device__ __forceinline__ u32x4 make_srd(const void* p, unsigned bytes) {
  const unsigned long long a = (unsigned long long)p;
  u32x4 s;
  s[0] = __builtin_amdgcn_readfirstlane((unsigned)a);
  s[1] = __builtin_amdgcn_readfirstlane((unsigned)(a >> 32));
  s[2] = bytes;
  s[3] = 0x00020000u;
  return s;
}
__device__ __forceinline__ void dma4k(unsigned m0, unsigned v0, unsigned v1, unsigned v2, unsigned v3, u32x4 srd, unsigned soff) {
  asm volatile("s_mov_b32 m0, %0\n\ts_nop 0\n\t"
               "buffer_load_dwordx4 %1, %5, %6 offen nt lds\n\t"
               "buffer_load_dwordx4 %2, %5, %6 offen offset:1024 nt lds\n\t"
               "buffer_load_dwordx4 %3, %5, %6 offen offset:2048 nt lds\n\t"
               "buffer_load_dwordx4 %4, %5, %6 offen offset:3072 nt lds"
               :: "s"(m0), "v"(v0), "v"(v1), "v"(v2), "v"(v3), "s"(srd), "s"(soff) : "memory");
}
__device__ __forceinline__ void dma1k(unsigned m0, unsigned v0, u32x4 srd, unsigned soff) {
  asm volatile("s_mov_b32 m0, %0\n\ts_nop 0\n\tbuffer_load_dwordx4 %1, %2, %3 offen nt lds"
               :: "s"(m0), "v"(v0), "s"(srd), "s"(soff) : "memory");
}
template <int N> __device__ __forceinline__ void wait_vm() { asm volatile("s_waitcnt vmcnt(%0)" :: "i"(N) : "memory"); }
__device__ __forceinline__ void wait_lgkm0() { asm volatile("s_waitcnt lgkmcnt(0)" ::: "memory"); }
__device__ __forceinline__ s16x4 tr_read(lds_ptr p) {
  typedef short v4i16_t __attribute__((ext_vector_type(4)));
  return __builtin_amdgcn_ds_read_tr16_b64_v4i16((LAS v4i16_t*)p);
}

__device__ __forceinline__ void store_out(__hip_bfloat16* out, int head, int w, int lane, const f32x4 (&acc)[NQD], float L) {
  const float inv = 1.0f / L;
  const int q = lane & 31, h = lane >> 5;
  char* orow = (char*)out + ((size_t)head * 32 + q) * (kD * 2);
#pragma unroll
  for (int j = 0; j < NQD; j += 2) {
    const unsigned ax = pack_bf16(acc[j][0] * inv, acc[j][1] * inv), ay = pack_bf16(acc[j][2] * inv, acc[j][3] * inv);
    const unsigned bx = pack_bf16(acc[j + 1][0] * inv, acc[j + 1][1] * inv), by = pack_bf16(acc[j + 1][2] * inv, acc[j + 1][3] * inv);
    auto rx = __builtin_amdgcn_permlane32_swap(ax, bx, false, false);
    auto ry = __builtin_amdgcn_permlane32_swap(ay, by, false, false);
    const int Qd = w * NQD + j;
    const u32x4 o = {rx[0], ry[0], rx[1], ry[1]};
    *(u32x4*)(orow + (Qd * 8 + 8 * h) * 2) = o;
  }
}

__global__ __launch_bounds__(W * 64, W / 4) void gqa_split_kernel(const __hip_bfloat16* __restrict__ Q, const __hip_bfloat16* __restrict__ K,
                                                                   const __hip_bfloat16* __restrict__ V, char* __restrict__ ws) {
  __shared__ __attribute__((aligned(1024))) char lds_raw[LDS_BYTES];
  const lds_ptr lds = (lds_ptr)lds_raw;
  const unsigned lds0 = (unsigned)(size_t)lds;
  const int lane = threadIdx.x & 63;
  const int w = __builtin_amdgcn_readfirstlane(threadIdx.x >> 6);
  const int head = blockIdx.x & (NHEAD - 1), split = blockIdx.x >> 7;
  const int r0 = split * (ROUNDS / 2), nt = ROUNDS / 2;

  const u32x4 srdK = make_srd((const char*)K + (size_t)head * HEAD_BYTES, HEAD_BYTES);
  const u32x4 srdV = make_srd((const char*)V + (size_t)head * HEAD_BYTES, HEAD_BYTES);

  const unsigned rip = lane >> 4, cpos = lane & 15;
  unsigned voK[4];
#pragma unroll
  for (int j = 0; j < 4; ++j) voK[j] = rip * 256 + ((cpos ^ ((4 * j + rip) & 15)) << 4);
  const unsigned voV = rip * 256 + ((cpos ^ (rip << 2)) << 4);

  const unsigned ringb = lds0 + w * RING;
  auto issue = [&](int tile, int kind, unsigned pos) {
    const int half = kind & 1;
    unsigned soff = KPAT == 0 ? (unsigned)(((r0 + tile) * W + w) * 2 + half) * 4096u : (unsigned)(((r0 + tile) * 2 + half) * W + w) * 4096u;
    u32x4 srd = (kind & 2) ? srdV : srdK;
    const bool live = tile < nt;
    srd[2] = live ? HEAD_BYTES : 0u;
    soff = live ? soff : 0u;
    if (kind & 2) dma4k(ringb + pos, voV, voV, voV, voV, srd, soff);
    else dma4k(ringb + pos, voK[0], voK[1], voK[2], voK[3], srd, soff);
  };

  {
    const u32x4 srdQ = make_srd((const char*)Q + (size_t)head * 8192, 8192);
#pragma unroll
    for (int jj = 0; jj < 8 / W; ++jj) {
      const int piece = w * (8 / W) + jj;
      const unsigned vo = rip * 256 + ((cpos ^ ((4 * piece + rip) & 15)) << 4);
      dma1k(lds0 + LDS_Q + piece * 1024, vo, srdQ, piece * 1024);
    }
  }
#pragma unroll
  for (int c = 0; c < 2 * NS; ++c) issue(c >> 2, c & 3, c * 4096);

  const int r32 = lane & 31, h = lane >> 5;
  unsigned ka[8];
#pragma unroll
  for (int s = 0; s < 8; ++s) ka[s] = r32 * 256 + (((2 * s + h) ^ (r32 & 15)) << 4);
  unsigned va[4];
  {
    const unsigned blk = (lane >> 4) & 1, qq = (lane & 15) >> 2, p = lane & 3;
#pragma unroll
    for (int c = 0; c < 4; ++c) va[c] = (4 * h + qq) * 256 + (((4 * c + 2 * blk + (p >> 1)) ^ (qq << 2)) << 4) + 8 * (p & 1);
  }

  wait_vm<8 * NS>();
  __builtin_amdgcn_s_barrier();
  bf16x8 qf[8];
#pragma unroll
  for (int s = 0; s < 8; ++s) qf[s] = *(const LAS bf16x8*)(lds + LDS_Q + ka[s]);

  float m = -3.0e38f, l = 0.f;
  f32x16 o[4];
#pragma unroll
  for (int c = 0; c < 4; ++c) o[c] = f32x16{};

  const lds_ptr ring = lds + w * RING;
  unsigned pk = 0;
  for (int t = 0; t < nt; ++t) {
    unsigned pv = pk + 8192; if (pv >= (unsigned)RING) pv -= RING;
    wait_vm<8 * (NS - 1)>();
    bf16x8 kf[8];
#pragma unroll
    for (int s = 0; s < 8; ++s) kf[s] = *(const LAS bf16x8*)(ring + pk + ka[s]);
    wait_lgkm0();
    issue(t + ((0 + 2 * NS) >> 2), (0 + 2 * NS) & 3, pk);
    issue(t + ((1 + 2 * NS) >> 2), (1 + 2 * NS) & 3, pk + 4096);
    f32x16 sc = f32x16{};
#pragma unroll
    for (int s = 0; s < 8; ++s) sc = __builtin_amdgcn_mfma_f32_32x32x16_bf16(kf[s], qf[s], sc, 0, 0, 0);
    float tmax = fmaxf(fmaxf(sc[0], sc[1]), sc[2]);
#pragma unroll
    for (int i = 3; i < 15; i += 2) tmax = fmaxf(fmaxf(tmax, sc[i]), sc[i + 1]);
    tmax = swap_max(fmaxf(tmax, sc[15]));
    if (__builtin_amdgcn_ballot_w64(tmax > m) != 0) {
      const float mn = fmaxf(m, tmax);
      const float alpha = __builtin_amdgcn_exp2f((m - mn) * C2);
      m = mn;
      l *= alpha;
#pragma unroll
      for (int c = 0; c < 4; ++c) o[c] *= alpha;
    }
    const float nm = -m * C2;
    float ps = 0.f;
#pragma unroll
    for (int i = 0; i < 16; ++i) { sc[i] = __builtin_amdgcn_exp2f(__builtin_fmaf(sc[i], C2, nm)); ps += sc[i]; }
    l += ps;
    u32x4 pw0, pw1;
#pragma unroll
    for (int i = 0; i < 4; ++i) { pw0[i] = pack_bf16(sc[2 * i], sc[2 * i + 1]); pw1[i] = pack_bf16(sc[8 + 2 * i], sc[9 + 2 * i]); }
    const bf16x8 pb0 = __builtin_bit_cast(bf16x8, pw0), pb1 = __builtin_bit_cast(bf16x8, pw1);
    {
      wait_vm<8 * NS - 4>();
      s16x4 lo[4], hi[4];
#pragma unroll
      for (int c = 0; c < 4; ++c) { lo[c] = tr_read(ring + pv + va[c]); hi[c] = tr_read(ring + pv + va[c] + 2048); }
      wait_lgkm0();
      issue(t + ((2 + 2 * NS) >> 2), (2 + 2 * NS) & 3, pv);
#pragma unroll
      for (int c = 0; c < 4; ++c)
        o[c] = __builtin_amdgcn_mfma_f32_32x32x16_bf16(__builtin_shufflevector(lo[c], hi[c], 0, 1, 2, 3, 4, 5, 6, 7), pb0, o[c], 0, 0, 0);
    }
    {
      wait_vm<8 * NS - 4>();
      s16x4 lo[4], hi[4];
#pragma unroll
      for (int c = 0; c < 4; ++c) { lo[c] = tr_read(ring + pv + 4096 + va[c]); hi[c] = tr_read(ring + pv + 4096 + va[c] + 2048); }
      wait_lgkm0();
      issue(t + ((3 + 2 * NS) >> 2), (3 + 2 * NS) & 3, pv + 4096);
#pragma unroll
      for (int c = 0; c < 4; ++c)
        o[c] = __builtin_amdgcn_mfma_f32_32x32x16_bf16(__builtin_shufflevector(lo[c], hi[c], 0, 1, 2, 3, 4, 5, 6, 7), pb1, o[c], 0, 0, 0);
    }
    pk += 16384; if (pk >= (unsigned)RING) pk -= RING;
  }
  wait_vm<0>();

  l = swap_sum(l);
  if (h == 0) *(LAS f32x2*)(lds + LDS_ST + w * 256 + r32 * 8) = f32x2{m, l};
#pragma unroll
  for (int c = 0; c < 4; ++c)
#pragma unroll
    for (int g = 0; g < 4; ++g)
      *(LAS f32x4*)(ring + (4 * c + g) * 1024 + lane * 16) = f32x4{o[c][4 * g], o[c][4 * g + 1], o[c][4 * g + 2], o[c][4 * g + 3]};
  __syncthreads();
  float mu[W], lu[W], M = -3.0e38f;
#pragma unroll
  for (int u = 0; u < W; ++u) { const f32x2 s = *(const LAS f32x2*)(lds + LDS_ST + u * 256 + r32 * 8); mu[u] = s[0]; lu[u] = s[1]; M = fmaxf(M, s[0]); }
  float L = 0.f;
  f32x4 acc[NQD];
#pragma unroll
  for (int j = 0; j < NQD; ++j) acc[j] = f32x4{0.f, 0.f, 0.f, 0.f};
#pragma unroll
  for (int u = 0; u < W; ++u) {
    const float f = __builtin_amdgcn_exp2f((mu[u] - M) * C2);
    L = __builtin_fmaf(f, lu[u], L);
#pragma unroll
    for (int j = 0; j < NQD; ++j) acc[j] += f * *(const LAS f32x4*)(lds + u * RING + (w * NQD + j) * 1024 + lane * 16);
  }
  f32x4* po = (f32x4*)(ws + WS_PO) + (size_t)(head * 2 + split) * 1024;
#pragma unroll
  for (int j = 0; j < NQD; ++j) po[(w * NQD + j) * 64 + lane] = acc[j];
  if (w == 0 && h == 0) ((f32x2*)(ws + WS_PS))[(head * 2 + split) * 32 + r32] = f32x2{M, L};
}

__global__ __launch_bounds__(W * 64) void gqa_combine_kernel(const char* __restrict__ ws, __hip_bfloat16* __restrict__ out) {
  const int lane = threadIdx.x & 63, w = threadIdx.x >> 6, head = blockIdx.x;
  const f32x4* po = (const f32x4*)(ws + WS_PO) + (size_t)head * 2048;
  const f32x2* ps = (const f32x2*)(ws + WS_PS) + head * 64;
  const f32x2 sa = ps[lane & 31], sb = ps[32 + (lane & 31)];
  const float M = fmaxf(sa[0], sb[0]);
  const float fa = __builtin_amdgcn_exp2f((sa[0] - M) * C2), fb = __builtin_amdgcn_exp2f((sb[0] - M) * C2);
  const float L = fa * sa[1] + fb * sb[1];
  f32x4 acc[NQD];
#pragma unroll
  for (int j = 0; j < NQD; ++j) acc[j] = fa * po[(w * NQD + j) * 64 + lane] + fb * po[1024 + (w * NQD + j) * 64 + lane];
  store_out(out, head, w, lane, acc, L);
}

}

extern "C" void kernel_launch(void* const* d_in, const int* in_sizes, int n_in, void* d_out, int out_size, void* d_ws, size_t ws_size,
                              hipStream_t stream) {
  const __hip_bfloat16* q = (const __hip_bfloat16*)d_in[0];
  const __hip_bfloat16* k = (const __hip_bfloat16*)d_in[1];
  const __hip_bfloat16* v = (const __hip_bfloat16*)d_in[2];
  gqa_split_kernel<<<2 * NHEAD, W * 64, 0, stream>>>(q, k, v, (char*)d_ws);
  gqa_combine_kernel<<<NHEAD, W * 64, 0, stream>>>((const char*)d_ws, (__hip_bfloat16*)d_out);
}
```

```cpp
#include <hip/hip_runtime.h>
#include <hip/hip_bf16.h>
#include <stdint.h>

namespace {

constexpr int kB = 8, kHKV = 16, kSK = 8192, kD = 128;
constexpr int NHEAD = kB * kHKV;
constexpr int W = 2;
constexpr int TILES = kSK / 32;
constexpr int TILES_A = 126;
constexpr int TAIL = 6;
constexpr int NQD = 16 / W;
constexpr unsigned HEAD_BYTES = kSK * kD * 2;
constexpr float C2 = 0.08838834764831845f * 1.4426950408889634f;
constexpr int PAUSE = 24, PAUSE_MAX = 45;
constexpr int ROUND16 = 1792;
constexpr int GAIN = 26;
static_assert(TAIL >= 1 && TAIL < TILES_A / W, "tail");

constexpr int RING = 2 * 16384;
constexpr int LDS_Q = W * RING;
constexpr int LDS_P = LDS_Q + 8192;
constexpr int LDS_ST = LDS_P + 16384;
constexpr int LDS_BYTES = LDS_ST + (W + 1) * 256;
static_assert(LDS_BYTES <= 163840 && 2 * LDS_BYTES > 163840, "LDS map");

constexpr unsigned WS_ST = 16384, WS_FLAG = WS_ST + 256, WS_ACK = WS_FLAG + 256, WS_HEAD = WS_ACK + 256;
constexpr unsigned long long ACK_KEY = 0x9E3779B97F4A7C15ull;
constexpr int AUX_SC1 = 16;

typedef short bf16x8 __attribute__((ext_vector_type(8)));
typedef short s16x4 __attribute__((ext_vector_type(4)));
typedef float f32x16 __attribute__((ext_vector_type(16)));
typedef float f32x4 __attribute__((ext_vector_type(4)));
typedef float f32x2 __attribute__((ext_vector_type(2)));
typedef unsigned u32x4 __attribute__((ext_vector_type(4)));
typedef unsigned u32x2 __attribute__((ext_vector_type(2)));
typedef __bf16 bf16x2_t __attribute__((ext_vector_type(2)));
#define LAS __attribute__((address_space(3)))
#define GAS __attribute__((address_space(1)))
typedef LAS char* lds_ptr;
typedef GAS unsigned long long* gu64_ptr;

__device__ __forceinline__ unsigned pack_bf16(float lo, float hi) {
  f32x2 v = {lo, hi};
  return __builtin_bit_cast(unsigned, __builtin_convertvector(v, bf16x2_t));
}
__device__ __forceinline__ bf16x8 pack8(const f32x16& p, int i0) {
  u32x4 pw;
#pragma unroll
  for (int i = 0; i < 4; ++i) pw[i] = pack_bf16(p[i0 + 2 * i], p[i0 + 2 * i + 1]);
  return __builtin_bit_cast(bf16x8, pw);
}
__device__ __forceinline__ float swap_max(float x) {
  auto r = __builtin_amdgcn_permlane32_swap(__float_as_uint(x), __float_as_uint(x), false, false);
  return fmaxf(__uint_as_float(r[0]), __uint_as_float(r[1]));
}
__device__ __forceinline__ float swap_sum(float x) {
  auto r = __builtin_amdgcn_permlane32_swap(__float_as_uint(x), __float_as_uint(x), false, false);
  return __uint_as_float(r[0]) + __uint_as_float(r[1]);
}
__device__ __forceinline__ f32x16 mfma(bf16x8 a, bf16x8 b, f32x16 c) { return __builtin_amdgcn_mfma_f32_32x32x16_bf16(a, b, c, 0, 0, 0); }
__device__ __forceinline__ void fence_sched() { __builtin_amdgcn_sched_barrier(0); }
__device__ __forceinline__ unsigned now_ticks() { return (unsigned)__builtin_amdgcn_s_memrealtime(); }
__device__ __forceinline__ u32x4 make_srd(const void* p, unsigned bytes) {
  const unsigned long long a = (unsigned long long)p;
  u32x4 s;
  s[0] = __builtin_amdgcn_readfirstlane((unsigned)a);
  s[1] = __builtin_amdgcn_readfirstlane((unsigned)(a >> 32));
  s[2] = bytes;
  s[3] = 0x00020000u;
  return s;
}
__device__ __forceinline__ void dma4k(unsigned m0, unsigned v0, unsigned v1, unsigned v2, unsigned v3, u32x4 srd, unsigned soff) {
  asm volatile("s_mov_b32 m0, %0\n\ts_nop 0\n\t"
               "buffer_load_dwordx4 %1, %5, %6 offen nt lds\n\t"
               "buffer_load_dwordx4 %2, %5, %6 offen offset:1024 nt lds\n\t"
               "buffer_load_dwordx4 %3, %5, %6 offen offset:2048 nt lds\n\t"
               "buffer_load_dwordx4 %4, %5, %6 offen offset:3072 nt lds"
               :: "s"(m0), "v"(v0), "v"(v1), "v"(v2), "v"(v3), "s"(srd), "s"(soff) : "memory");
}
__device__ __forceinline__ void dma1k(unsigned m0, unsigned v0, u32x4 srd, unsigned soff) {
  asm volatile("s_mov_b32 m0, %0\n\ts_nop 0\n\tbuffer_load_dwordx4 %1, %2, %3 offen nt lds"
               :: "s"(m0), "v"(v0), "s"(srd), "s"(soff) : "memory");
}
template <int N> __device__ __forceinline__ void wait_vm() { asm volatile("s_waitcnt vmcnt(%0)" :: "i"(N) : "memory"); }
__device__ __forceinline__ void wait_lgkm0() { asm volatile("s_waitcnt lgkmcnt(0)" ::: "memory"); }
__device__ __forceinline__ void barrier() { asm volatile("s_barrier" ::: "memory"); }
__device__ __forceinline__ s16x4 tr_read(lds_ptr p) {
  typedef short v4i16_t __attribute__((ext_vector_type(4)));
  return __builtin_amdgcn_ds_read_tr16_b64_v4i16((LAS v4i16_t*)p);
}
__device__ __forceinline__ bf16x8 tr_frag(lds_ptr p) {
  const s16x4 lo = tr_read(p), hi = tr_read(p + 2048);
  return __builtin_shufflevector(lo, hi, 0, 1, 2, 3, 4, 5, 6, 7);
}
__device__ __forceinline__ void pin8(bf16x8 (&f)[8]) {
  asm volatile("s_waitcnt lgkmcnt(0)" : "+v"(f[0]), "+v"(f[1]), "+v"(f[2]), "+v"(f[3]), "+v"(f[4]), "+v"(f[5]), "+v"(f[6]), "+v"(f[7]) :: "memory");
}
__device__ __forceinline__ void pin4(bf16x8 (&f)[4]) {
  asm volatile("s_waitcnt lgkmcnt(0)" : "+v"(f[0]), "+v"(f[1]), "+v"(f[2]), "+v"(f[3]) :: "memory");
}
__device__ __forceinline__ void pin8(f32x4 (&f)[8]) {
  asm volatile("s_waitcnt lgkmcnt(0)" : "+v"(f[0]), "+v"(f[1]), "+v"(f[2]), "+v"(f[3]), "+v"(f[4]), "+v"(f[5]), "+v"(f[6]), "+v"(f[7]) :: "memory");
}
__device__ __forceinline__ unsigned long long ld_word(gu64_ptr p) {
  const unsigned long long x = __hip_atomic_load(p, __ATOMIC_RELAXED, __HIP_MEMORY_SCOPE_AGENT);
  return ((unsigned long long)__builtin_amdgcn_readfirstlane((unsigned)(x >> 32)) << 32) | (unsigned)__builtin_amdgcn_readfirstlane((unsigned)x);
}
template <bool EQ> __device__ __forceinline__ void poll_word(gu64_ptr p, unsigned long long x) {
  for (unsigned spins = 0; spins < (1u << 18); ++spins) {
    if ((ld_word(p) == x) == EQ) break;
    __builtin_amdgcn_s_sleep(4);
  }
}

template <int WI>
__device__ __forceinline__ void finish(const f32x16 (&o)[4], float m, float l, lds_ptr lds, int lane, bool fin, int head, char* ws, __hip_bfloat16* out) {
  const int r32 = lane & 31, h = lane >> 5;
  if (h == 0) *(LAS f32x2*)(lds + LDS_ST + WI * 256 + r32 * 8) = f32x2{m, l};
#pragma unroll
  for (int Qd = 0; Qd < 16; ++Qd)
    if (Qd / NQD != WI) {
      const int c = Qd >> 2, g = Qd & 3;
      *(LAS f32x4*)(lds + WI * RING + Qd * 1024 + lane * 16) = f32x4{o[c][4 * g], o[c][4 * g + 1], o[c][4 * g + 2], o[c][4 * g + 3]};
    }
  wait_lgkm0();
  barrier();
  constexpr int WO = 1 - WI;
  f32x2 st[W + 1];
  f32x4 xo[NQD], xp[NQD];
#pragma unroll
  for (int u = 0; u <= W; ++u) st[u] = *(const LAS f32x2*)(lds + LDS_ST + (u < W || fin ? u : 0) * 256 + r32 * 8);
#pragma unroll
  for (int j = 0; j < NQD; ++j) xo[j] = *(const LAS f32x4*)(lds + WO * RING + (WI * NQD + j) * 1024 + lane * 16);
  if (fin) {
#pragma unroll
    for (int j = 0; j < NQD; ++j) xp[j] = *(const LAS f32x4*)(lds + LDS_P + (WI * NQD + j) * 1024 + lane * 16);
    pin8(xp);
  }
  pin8(xo);
  if (!fin) st[W] = f32x2{-3.0e38f, 0.f};
  const float M = fmaxf(fmaxf(st[0][0], st[1][0]), st[W][0]);
  float L = 0.f, f[W + 1];
#pragma unroll
  for (int u = 0; u <= W; ++u) { f[u] = __builtin_amdgcn_exp2f((st[u][0] - M) * C2); L = __builtin_fmaf(f[u], st[u][1], L); }
  if (fin) {
    const float inv = 1.0f / L;
#pragma unroll
    for (int u = 0; u <= W; ++u) f[u] *= inv;
  }
  f32x4 acc[NQD];
#pragma unroll
  for (int j = 0; j < NQD; ++j) {
    const int Qd = WI * NQD + j, c = Qd >> 2, g = Qd & 3;
    const f32x4 own = f32x4{o[c][4 * g], o[c][4 * g + 1], o[c][4 * g + 2], o[c][4 * g + 3]};
    acc[j] = WI == 0 ? f[0] * own + f[1] * xo[j] : f[0] * xo[j] + f[1] * own;
  }
  if (fin) {
    char* orow = (char*)out + ((size_t)head * 32 + r32) * (kD * 2);
#pragma unroll
    for (int j = 0; j < NQD; j += 2) {
      const f32x4 a = acc[j] + f[W] * xp[j], b = acc[j + 1] + f[W] * xp[j + 1];
      const unsigned ax = pack_bf16(a[0], a[1]), ay = pack_bf16(a[2], a[3]);
      const unsigned bx = pack_bf16(b[0], b[1]), by = pack_bf16(b[2], b[3]);
      auto rx = __builtin_amdgcn_permlane32_swap(ax, bx, false, false);
      auto ry = __builtin_amdgcn_permlane32_swap(ay, by, false, false);
      const u32x4 ov = {rx[0], ry[0], rx[1], ry[1]};
      *(u32x4*)(orow + ((WI * NQD + j) * 8 + 8 * h) * 2) = ov;
    }
  } else {
    const __amdgpu_buffer_rsrc_t rs = __builtin_amdgcn_make_buffer_rsrc(ws + (size_t)head * WS_HEAD, 0, WS_FLAG, 0x00020000);
#pragma unroll
    for (int j = 0; j < NQD; ++j)
      __builtin_amdgcn_raw_buffer_store_b128(__builtin_bit_cast(u32x4, acc[j]), rs, (WI * NQD + j) * 1024 + lane * 16, 0, AUX_SC1);
    if (WI == 0 && h == 0) __builtin_amdgcn_raw_buffer_store_b64(__builtin_bit_cast(u32x2, f32x2{M, L}), rs, WS_ST + r32 * 8, 0, AUX_SC1);
    wait_vm<0>();
    barrier();
  }
}

__global__ __launch_bounds__((W + 1) * 64, 2) void gqa_kernel(const __hip_bfloat16* __restrict__ Q, const __hip_bfloat16* __restrict__ K,
                                                              const __hip_bfloat16* __restrict__ V, char* __restrict__ ws,
                                                              __hip_bfloat16* __restrict__ out) {
  extern __shared__ __attribute__((aligned(1024))) char lds_raw[];
  const lds_ptr lds = (lds_ptr)lds_raw;
  const unsigned lds0 = (unsigned)(size_t)lds;
  const int lane = threadIdx.x & 63;
  const int w = __builtin_amdgcn_readfirstlane(threadIdx.x >> 6);
  const int head = blockIdx.x & (NHEAD - 1);
  const bool fin = blockIdx.x >= NHEAD;

  if (w == W) {
    barrier();
    const gu64_ptr flag = (gu64_ptr)(ws + (size_t)head * WS_HEAD + WS_FLAG), ack = (gu64_ptr)(ws + (size_t)head * WS_HEAD + WS_ACK);
    const unsigned long long x = ld_word(flag);
    if (fin) {
      if (lane == 0) __hip_atomic_store(ack, x + ACK_KEY, __ATOMIC_RELAXED, __HIP_MEMORY_SCOPE_AGENT);
      poll_word<false>(flag, x);
      const __amdgpu_buffer_rsrc_t rs = __builtin_amdgcn_make_buffer_rsrc(ws + (size_t)head * WS_HEAD, 0, WS_FLAG, 0x00020000);
      u32x4 p[16];
#pragma unroll
      for (int j = 0; j < 16; ++j) p[j] = __builtin_amdgcn_raw_buffer_load_b128(rs, j * 1024 + lane * 16, 0, AUX_SC1);
      const u32x2 st = __builtin_amdgcn_raw_buffer_load_b64(rs, WS_ST + (lane & 31) * 8, 0, AUX_SC1);
#pragma unroll
      for (int j = 0; j < 16; ++j) *(LAS u32x4*)(lds + LDS_P + j * 1024 + lane * 16) = p[j];
      *(LAS u32x2*)(lds + LDS_ST + W * 256 + (lane & 31) * 8) = st;
      wait_lgkm0();
      barrier();
    } else {
      poll_word<true>(ack, x + ACK_KEY);
      barrier();
      barrier();
      if (lane == 0) __hip_atomic_store(flag, x + 1, __ATOMIC_RELAXED, __HIP_MEMORY_SCOPE_AGENT);
    }
    return;
  }

  const int tile0 = (fin ? TILES_A : 0) + w, nt = ((fin ? TILES - TILES_A : TILES_A) - w + W - 1) / W;
  const u32x4 srdK = make_srd((const char*)K + (size_t)head * HEAD_BYTES, HEAD_BYTES);
  const u32x4 srdV = make_srd((const char*)V + (size_t)head * HEAD_BYTES, HEAD_BYTES);

  const unsigned rip = lane >> 4, cpos = lane & 15;
  unsigned voK[4];
#pragma unroll
  for (int j = 0; j < 4; ++j) voK[j] = rip * 256 + ((cpos ^ ((4 * j + rip) & 15)) << 4);
  const unsigned voV = rip * 256 + ((cpos ^ (rip << 2)) << 4);

  const unsigned ringb = lds0 + w * RING;
  auto issue = [&](int t, int kind) {
    unsigned soff = ((unsigned)(tile0 + t * W) * 2 + (kind & 1)) * 4096u;
    u32x4 srd = (kind & 2) ? srdV : srdK;
    const bool live = t < nt;
    srd[2] = live ? HEAD_BYTES : 0u;
    soff = live ? soff : 0u;
    const unsigned dst = ringb + (t & 1) * 16384 + kind * 4096;
    if (kind & 2) dma4k(dst, voV, voV, voV, voV, srd, soff);
    else dma4k(dst, voK[0], voK[1], voK[2], voK[3], srd, soff);
  };

  {
    const u32x4 srdQ = make_srd((const char*)Q + (size_t)head * 8192, 8192);
#pragma unroll
    for (int jj = 0; jj < 8 / W; ++jj) {
      const int piece = w * (8 / W) + jj;
      const unsigned vo = rip * 256 + ((cpos ^ ((4 * piece + rip) & 15)) << 4);
      dma1k(lds0 + LDS_Q + piece * 1024, vo, srdQ, piece * 1024);
    }
  }
#pragma unroll
  for (int c = 0; c < 4; ++c) issue(0, c);

  const int r32 = lane & 31, h = lane >> 5;
  unsigned ka[8];
#pragma unroll
  for (int s = 0; s < 8; ++s) ka[s] = r32 * 256 + (((2 * s + h) ^ (r32 & 15)) << 4);
  unsigned va[4];
  {
    const unsigned blk = (lane >> 4) & 1, qq = (lane & 15) >> 2, p = lane & 3;
#pragma unroll
    for (int c = 0; c < 4; ++c) va[c] = (4 * h + qq) * 256 + (((4 * c + 2 * blk + (p >> 1)) ^ (qq << 2)) << 4) + 8 * (p & 1);
  }

  wait_vm<16>();
  barrier();
  bf16x8 qf[8];
#pragma unroll
  for (int s = 0; s < 8; ++s) qf[s] = *(const LAS bf16x8*)(lds + LDS_Q + ka[s]);

  float m = -3.0e38f, l = 0.f;
  f32x16 o[4];
#pragma unroll
  for (int c = 0; c < 4; ++c) o[c] = f32x16{};

  const lds_ptr ring = lds + w * RING;
  auto read_k = [&](bf16x8 (&kf)[8], int t) {
#pragma unroll
    for (int s = 0; s < 8; ++s) kf[s] = *(const LAS bf16x8*)(ring + (t & 1) * 16384 + ka[s]);
  };
  auto read_v = [&](bf16x8 (&vf)[4], int t, int half) {
#pragma unroll
    for (int c = 0; c < 4; ++c) vf[c] = tr_frag(ring + (t & 1) * 16384 + 8192 + half * 4096 + va[c]);
  };
  auto new_max = [&](const f32x16& sc) {
    float tmax = fmaxf(fmaxf(sc[0], sc[1]), sc[2]);
#pragma unroll
    for (int i = 3; i < 15; i += 2) tmax = fmaxf(fmaxf(tmax, sc[i]), sc[i + 1]);
    tmax = swap_max(fmaxf(tmax, sc[15]));
    if (__builtin_amdgcn_ballot_w64(tmax > m) != 0) {
      const float mn = fmaxf(m, tmax);
      const float alpha = __builtin_amdgcn_exp2f((m - mn) * C2);
      m = mn;
      l *= alpha;
#pragma unroll
      for (int c = 0; c < 4; ++c) o[c] *= alpha;
    }
  };
  auto accumulate = [&](const f32x16& p, const bf16x8 (&v0)[4], const bf16x8 (&v1)[4]) {
    const bf16x8 pb0 = pack8(p, 0), pb1 = pack8(p, 8);
#pragma unroll
    for (int c = 0; c < 4; ++c) o[c] = mfma(v0[c], pb0, o[c]);
#pragma unroll
    for (int c = 0; c < 4; ++c) o[c] = mfma(v1[c], pb1, o[c]);
    float ps = 0.f;
#pragma unroll
    for (int i = 0; i < 16; ++i) ps += p[i];
    l += ps;
  };

  int t = 0;
  unsigned t_first = 0;
  for (; t < nt - TAIL; ++t) {
    wait_vm<8>();
    const unsigned t_in = now_ticks();
    bf16x8 kf[8], v0[4], v1[4];
    read_k(kf, t);
    pin8(kf);
    if (t == 0) t_first = t_in;
    const int ahead = t * ROUND16 - (int)((t_in - t_first) << 4);
    const unsigned pause = min((unsigned)PAUSE + ((min((unsigned)max(ahead, 0), 1u << 16) * GAIN) >> 12), (unsigned)PAUSE_MAX);
    while (now_ticks() - t_in < pause) { }
    issue(t + 1, 0);
    issue(t + 1, 1);
    f32x16 sc = f32x16{};
    fence_sched();
#pragma unroll
    for (int s = 0; s < 2; ++s) sc = mfma(kf[s], qf[s], sc);
    fence_sched();
    wait_vm<12>();
    read_v(v0, t, 0);
    fence_sched();
#pragma unroll
    for (int s = 2; s < 5; ++s) sc = mfma(kf[s], qf[s], sc);
    fence_sched();
    pin4(v0);
    issue(t + 1, 2);
    wait_vm<12>();
    read_v(v1, t, 1);
    fence_sched();
#pragma unroll
    for (int s = 5; s < 8; ++s) sc = mfma(kf[s], qf[s], sc);
    fence_sched();
    pin4(v1);
    issue(t + 1, 3);
    fence_sched();
    new_max(sc);
    const float nm = -m * C2;
#pragma unroll
    for (int i = 0; i < 16; ++i) sc[i] = __builtin_amdgcn_exp2f(__builtin_fmaf(sc[i], C2, nm));
    accumulate(sc, v0, v1);
  }

#pragma unroll
  for (int c = 0; c < 4; ++c) issue(t + 1, c);
  f32x16 sc = f32x16{};
  {
    wait_vm<24>();
    bf16x8 kf[8];
    read_k(kf, t);
    pin8(kf);
    issue(t + 2, 0);
    issue(t + 2, 1);
    fence_sched();
#pragma unroll
    for (int s = 0; s < 8; ++s) sc = mfma(kf[s], qf[s], sc);
  }
  const int t0 = t;
  for (; t < nt - 1; ++t) {
    if (t < t0 + 2) wait_vm<16>(); else wait_vm<24>();
    bf16x8 kf[8], v0[4], v1[4];
    read_k(kf, t + 1);
    new_max(sc);
    pin8(kf);
    issue(t + 3, 0);
    issue(t + 3, 1);
    const float nm = -m * C2;
    f32x16 sn = f32x16{};
    auto step = [&](int s) {
      sn = mfma(kf[s], qf[s], sn);
      asm volatile("" : "+v"(sn));
      sc[2 * s] = __builtin_amdgcn_exp2f(__builtin_fmaf(sc[2 * s], C2, nm));
      sc[2 * s + 1] = __builtin_amdgcn_exp2f(__builtin_fmaf(sc[2 * s + 1], C2, nm));
      fence_sched();
    };
    fence_sched();
    step(0);
    wait_vm<28>();
    read_v(v0, t, 0);
    step(1);
    step(2);
    step(3);
    pin4(v0);
    issue(t + 2, 2);
    wait_vm<28>();
    read_v(v1, t, 1);
    step(4);
    step(5);
    step(6);
    pin4(v1);
    issue(t + 2, 3);
    step(7);
    accumulate(sc, v0, v1);
    sc = sn;
  }
  {
    wait_vm<16>();
    bf16x8 v0[4], v1[4];
    read_v(v0, t, 0);
    read_v(v1, t, 1);
    new_max(sc);
    const float nm = -m * C2;
#pragma unroll
    for (int i = 0; i < 16; ++i) sc[i] = __builtin_amdgcn_exp2f(__builtin_fmaf(sc[i], C2, nm));
    accumulate(sc, v0, v1);
  }
  wait_vm<0>();

  l = swap_sum(l);
  if (w == 0) finish<0>(o, m, l, lds, lane, fin, head, ws, out);
  else finish<1>(o, m, l, lds, lane, fin, head, ws, out);
}

}

extern "C" void kernel_launch(void* const* d_in, const int* in_sizes, int n_in, void* d_out, int out_size, void* d_ws, size_t ws_size,
                              hipStream_t stream) {
  const __hip_bfloat16* q = (const __hip_bfloat16*)d_in[0];
  const __hip_bfloat16* k = (const __hip_bfloat16*)d_in[1];
  const __hip_bfloat16* v = (const __hip_bfloat16*)d_in[2];
  (void)hipFuncSetAttribute((const void*)gqa_kernel, hipFuncAttributeMaxDynamicSharedMemorySize, LDS_BYTES);
  gqa_kernel<<<2 * NHEAD, (W + 1) * 64, LDS_BYTES, stream>>>(q, k, v, (char*)d_ws, (__hip_bfloat16*)d_out);
}
```
